```python
import torch

def _softmax(x, axis=-1):
    return torch.softmax(x, dim=axis)
def _gelu(x):
    return torch.nn.functional.gelu(x, approximate="tanh")
def _silu(x):
    return torch.nn.functional.silu(x)
def _sigmoid(x):
    return torch.sigmoid(x)
def _layernorm(x, axis=-1, eps=1e-5):
    m = x.mean(dim=axis, keepdim=True)
    v = x.var(dim=axis, keepdim=True, unbiased=False)
    return (x - m) / torch.sqrt(v + eps)
def _scatter_add(tgt, idx, src, axis=0):
    return tgt.clone().index_add_(axis, idx, src)
def _segment_sum(src, seg, n):
    out = torch.zeros((n,) + src.shape[1:], dtype=src.dtype)
    return out.index_add_(0, seg, src)
def _pad(x, pw):
    flat = tuple(v for pair in reversed(pw) for v in pair)
    return torch.nn.functional.pad(x, flat)

kernel_name = 'concat_heads__compressed_kv_cla__compressed_kv_cla__decode'

def setup_inputs():

    _w = (lambda a: (lambda t: t.to(torch.bfloat16) if t.is_floating_point() else t)(torch.as_tensor(a)))
    def _A_setup():
        B, H, Sq, Sk, D, M = 2, 16, 1, 4096, 256, 448
        Dc = 4 * D
        return {
            "x": _w((lambda *s: torch.randn(*s, dtype=torch.bfloat16) * 3.2)(B, Sq, M)),
            "c_kv": _w((lambda *s: torch.randn(*s, dtype=torch.bfloat16) * 0.05)(B, Sk, Dc)),
            "w_q": _w((lambda *s: torch.randn(*s, dtype=torch.bfloat16) * 3.2)(M, H, D) * (M ** -0.5)),
            "w_uk": _w((lambda *s: torch.randn(*s, dtype=torch.bfloat16) * 3.2)(Dc, H, D) * (Dc ** -0.5)),
            "w_uv": _w((lambda *s: torch.randn(*s, dtype=torch.bfloat16) * 0.05)(Dc, H, D) * (Dc ** -0.5)),
            "w_o": _w((lambda *s: torch.randn(*s, dtype=torch.bfloat16) * 0.05)(H, D, M) * ((H * D) ** -0.5)),
        }
    _A_in = _A_setup()

    def _B_setup():
        B, H, Sq, Sk, D, M = 2, 16, 1, 4096, 256, 448
        Dc = 4 * D
        return {
            "x": _w((lambda *s: torch.randn(*s, dtype=torch.bfloat16) * 3.2)(B, Sq, M)),
            "c_kv": _w((lambda *s: torch.randn(*s, dtype=torch.bfloat16) * 0.05)(B, Sk, Dc)),
            "w_q": _w((lambda *s: torch.randn(*s, dtype=torch.bfloat16) * 3.2)(M, H, D) * (M ** -0.5)),
            "w_uk": _w((lambda *s: torch.randn(*s, dtype=torch.bfloat16) * 3.2)(Dc, H, D) * (Dc ** -0.5)),
            "w_uv": _w((lambda *s: torch.randn(*s, dtype=torch.bfloat16) * 0.05)(Dc, H, D) * (Dc ** -0.5)),
            "w_o": _w((lambda *s: torch.randn(*s, dtype=torch.bfloat16) * 0.05)(H, D, M) * ((H * D) ** -0.5)),
        }
    _B_in = _B_setup()

    return {
        "A_x": _A_in["x"],
        "A_c_kv": _A_in["c_kv"],
        "A_w_q": _A_in["w_q"],
        "A_w_uk": _A_in["w_uk"],
        "A_w_uv": _A_in["w_uv"],
        "A_w_o": _A_in["w_o"],
        "B_x": _B_in["x"],
        "B_c_kv": _B_in["c_kv"],
        "B_w_q": _B_in["w_q"],
        "B_w_uk": _B_in["w_uk"],
        "B_w_uv": _B_in["w_uv"],
        "B_w_o": _B_in["w_o"],
    }

def reference(A_x, A_c_kv, A_w_q, A_w_uk, A_w_uv, A_w_o, B_x, B_c_kv, B_w_q, B_w_uk, B_w_uv, B_w_o):
    def _A_ref(x, c_kv, w_q, w_uk, w_uv, w_o):
        B, Sq, M = x.shape[0], x.shape[1], x.shape[2]
        Sk = c_kv.shape[1]
        H, D = w_q.shape[1], w_q.shape[2]
        q = torch.einsum("bqm,mhd->bhqd", x, w_q)
        k = torch.einsum("bkc,chd->bhkd", c_kv, w_uk)
        v = torch.einsum("bkc,chd->bhkd", c_kv, w_uv)
        scores = torch.einsum("bhqd,bhkd->bhqk", q, k) * (D ** -0.5)
        off = Sk - Sq
        qi = torch.arange(Sq)[:, None]
        kj = torch.arange(Sk)[None, :]
        cmask = kj <= (qi + off)
        scores = torch.where(cmask[None, None], scores, -1e9)
        p = _softmax(scores, axis=-1)
        ctx = torch.einsum("bhqk,bhkd->bhqd", p, v)
        return torch.einsum("bhqd,hdm->bqm", ctx, w_o)

    def _B_ref(x, c_kv, w_q, w_uk, w_uv, w_o):
        B, Sq, M = x.shape[0], x.shape[1], x.shape[2]
        Sk = c_kv.shape[1]
        H, D = w_q.shape[1], w_q.shape[2]
        q = torch.einsum("bqm,mhd->bhqd", x, w_q)
        k = torch.einsum("bkc,chd->bhkd", c_kv, w_uk)
        v = torch.einsum("bkc,chd->bhkd", c_kv, w_uv)
        scores = torch.einsum("bhqd,bhkd->bhqk", q, k) * (D ** -0.5)
        off = Sk - Sq
        qi = torch.arange(Sq)[:, None]
        kj = torch.arange(Sk)[None, :]
        cmask = kj <= (qi + off)
        scores = torch.where(cmask[None, None], scores, -1e9)
        p = _softmax(scores, axis=-1)
        ctx = torch.einsum("bhqk,bhkd->bhqd", p, v)
        return torch.einsum("bhqd,hdm->bqm", ctx, w_o)

    a = _A_ref(A_x, A_c_kv, A_w_q, A_w_uk, A_w_uv, A_w_o)
    b = _B_ref(B_x, B_c_kv, B_w_q, B_w_uk, B_w_uv, B_w_o)
    return torch.concatenate([a, b], axis=-1)
```

```cpp
#include <hip/hip_runtime.h>
#include <hip/hip_bf16.h>
typedef unsigned v4u __attribute__((ext_vector_type(4)));
typedef float f32x4 __attribute__((ext_vector_type(4)));
typedef short bf16x8 __attribute__((ext_vector_type(8)));
typedef unsigned short u16;

constexpr int H=16, SK=4096, D=256, M=448, DC=1024;
struct P { const u16* x[2]; const u16* ckv[2]; const u16* wq[2]; const u16* wuk[2]; const u16* wuv[2]; const u16* wo[2]; u16* out; unsigned char* ws; };

__device__ __forceinline__ float bflo(unsigned u){ return __uint_as_float(u<<16); }
__device__ __forceinline__ float bfhi(unsigned u){ return __uint_as_float(u & 0xffff0000u); }
__device__ __forceinline__ u16 f2bf(float f){ __hip_bfloat16 h = __float2bfloat16(f); return *(u16*)&h; }
__device__ __forceinline__ f32x4 mfma(v4u a, v4u b, f32x4 c) {
  return __builtin_amdgcn_mfma_f32_16x16x32_bf16(__builtin_bit_cast(bf16x8, a), __builtin_bit_cast(bf16x8, b), c, 0, 0, 0);
}
constexpr size_t WS_Q = 0;
constexpr size_t WS_QA = 65536;
constexpr size_t WS_ML = 262144;
constexpr size_t WS_CP = 393216;
constexpr size_t WS_OP = 1048576;
constexpr size_t WS_O = 2097152;

__global__ void __launch_bounds__(256) k1(P p) {
  __shared__ float red[64][2][33];
  const int br = blockIdx.x>>7, cb = blockIdx.x&127, t = threadIdx.x, ch = t&3, rg = t>>2;
  float a0[8]={0}, a1[8]={0};
#pragma unroll
  for (int i=0;i<7;++i){ int m = rg + 64*i;
    v4u w = *(const v4u*)(p.wq[br] + (size_t)m*4096 + cb*32 + ch*8);
    float x0 = bflo(p.x[br][m]), x1 = bflo(p.x[br][M+m]);
#pragma unroll
    for (int j=0;j<4;++j){ float lo=bflo(w[j]), hi=bfhi(w[j]); a0[2*j]+=x0*lo; a0[2*j+1]+=x0*hi; a1[2*j]+=x1*lo; a1[2*j+1]+=x1*hi; }
  }
#pragma unroll
  for (int j=0;j<8;++j){ red[rg][0][ch*8+j]=a0[j]; red[rg][1][ch*8+j]=a1[j]; }
  __syncthreads();
  if (t<64){ int b=t>>5, c=t&31; float s=0; for(int r=0;r<64;++r) s+=red[r][b][c];
    ((u16*)(p.ws+WS_Q))[(br*2+b)*4096 + cb*32 + c] = f2bf(s); }
}
__global__ void __launch_bounds__(256) k2(P p) {
  const int br = blockIdx.x>>7, h=(blockIdx.x>>3)&15, j=blockIdx.x&7, t=threadIdx.x, l=t&63, w=t>>6, r=l&15, g=l>>4;
  const u16* q = (const u16*)(p.ws+WS_Q) + br*2*4096 + h*256;
  v4u a[8];
#pragma unroll
  for (int s=0;s<8;++s){ v4u z={0,0,0,0}; a[s] = r<2 ? *(const v4u*)(q + r*4096 + s*32 + g*8) : z; }
#pragma unroll
  for (int tt=0;tt<2;++tt){
    int c = j*128 + w*32 + tt*16 + r;
    const u16* wp = p.wuk[br] + (size_t)c*4096 + h*256 + g*8;
    f32x4 acc={0,0,0,0};
#pragma unroll
    for (int s=0;s<8;++s){ v4u b = *(const v4u*)(wp + s*32); acc = mfma(a[s], b, acc); }
    if (g==0){ u16* qa=(u16*)(p.ws+WS_QA); qa[((br*2+0)*16+h)*1024 + c]=f2bf(acc[0]); qa[((br*2+1)*16+h)*1024+c]=f2bf(acc[1]); }
  }
}
__global__ void __launch_bounds__(256) k3(P p) {
  extern __shared__ __attribute__((aligned(16))) unsigned char sm[];
  u16* tile = (u16*)sm;
  float* S = (float*)(sm + 64*1032*2);
  const int br=blockIdx.x>>7, b=(blockIdx.x>>6)&1, kb=blockIdx.x&63, t=threadIdx.x, l=t&63, w=t>>6, r=l&15, g=l>>4;
  const u16* src = p.ckv[br] + ((size_t)b*SK + kb*64)*DC;
  for (int i=t;i<64*128;i+=256){ int row=i>>7, ch=i&127; *(v4u*)(tile+row*1032+ch*8) = *(const v4u*)(src+(size_t)row*DC+ch*8); }
  __syncthreads();
  const u16* qa=(const u16*)(p.ws+WS_QA) + (size_t)(br*2+b)*16*1024;
  f32x4 acc={0,0,0,0};
  for (int s=0;s<32;++s){ v4u a=*(const v4u*)(tile+(w*16+r)*1032+s*32+g*8); v4u bq=*(const v4u*)(qa+r*1024+s*32+g*8); acc=mfma(a,bq,acc); }
#pragma unroll
  for (int i=0;i<4;++i) S[r*64 + w*16+4*g+i] = acc[i]*0.0625f;
  __syncthreads();
  if (t<16){ float m=-INFINITY; for(int k=0;k<64;++k) m=fmaxf(m,S[t*64+k]); float s=0; for(int k=0;k<64;++k){ float e=__expf(S[t*64+k]-m); S[t*64+k]=e; s+=e; }
    float* ml=(float*)(p.ws+WS_ML)+(((br*2+b)*64+kb)*16+t)*2; ml[0]=m; ml[1]=s; }
  __syncthreads();
  float o[16][4];
#pragma unroll
  for(int h=0;h<16;++h){o[h][0]=o[h][1]=o[h][2]=o[h][3]=0;}
  for (int k=0;k<64;++k){ uint2 v=*(const uint2*)(tile+k*1032+t*4); float c0=bflo(v.x),c1=bfhi(v.x),c2=bflo(v.y),c3=bfhi(v.y);
#pragma unroll
    for(int h=0;h<16;++h){ float pp=S[h*64+k]; o[h][0]+=pp*c0;o[h][1]+=pp*c1;o[h][2]+=pp*c2;o[h][3]+=pp*c3; } }
  float* O=(float*)(p.ws+WS_O)+(size_t)((br*2+b)*64+kb)*16*1024;
#pragma unroll
  for(int h=0;h<16;++h){ f32x4 v={o[h][0],o[h][1],o[h][2],o[h][3]}; *(f32x4*)(O+h*1024+t*4)=v; }
}
__global__ void __launch_bounds__(256) k4(P p) {
  __shared__ float wgt[2][64]; __shared__ float cc[2][128]; __shared__ float red[8][2][256];
  const int br=blockIdx.x>>7,h=(blockIdx.x>>3)&15,j=blockIdx.x&7,t=threadIdx.x;
  if (t<128){ int b=t>>6,kb=t&63; const float* ml=(const float*)(p.ws+WS_ML)+(((br*2+b)*64+kb)*16+h)*2; float m=ml[0], l=ml[1];
    float M_=m; for(int o=32;o;o>>=1) M_=fmaxf(M_,__shfl_xor(M_,o)); float e=__expf(m-M_); float L=e*l; for(int o=32;o;o>>=1) L+=__shfl_xor(L,o); wgt[b][kb]=e/L; }
  __syncthreads();
  { int b=t>>7,c=t&127; const float* O=(const float*)(p.ws+WS_O)+((size_t)(br*2+b)*64*16+h)*1024 + j*128+c; float s=0;
    for(int kb=0;kb<64;++kb) s+=wgt[b][kb]*O[(size_t)kb*16*1024]; cc[b][c]=s; }
  __syncthreads();
  const int ch=t&31, rg=t>>5; float a0[8]={0},a1[8]={0};
#pragma unroll
  for(int i=0;i<16;++i){ int c=rg+8*i; v4u w=*(const v4u*)(p.wuv[br]+(size_t)(j*128+c)*4096+h*256+ch*8); float x0=cc[0][c],x1=cc[1][c];
#pragma unroll
    for (int q=0;q<4;++q){ float lo=bflo(w[q]), hi=bfhi(w[q]); a0[2*q]+=x0*lo; a0[2*q+1]+=x0*hi; a1[2*q]+=x1*lo; a1[2*q+1]+=x1*hi; } }
#pragma unroll
  for(int q=0;q<8;++q){ red[rg][0][ch*8+q]=a0[q]; red[rg][1][ch*8+q]=a1[q]; }
  __syncthreads();
  for(int i=t;i<512;i+=256){ int b=i>>8,d=i&255; float s=0; for(int r=0;r<8;++r) s+=red[r][b][d]; ((float*)(p.ws+WS_CP))[(((br*16+h)*8+j)*2+b)*256+d]=s; }
}
__global__ void __launch_bounds__(256) k5(P p) {
  __shared__ float cx[2][32]; __shared__ float red[4][2][448];
  const int br=blockIdx.x>>7,h=(blockIdx.x>>3)&15,j=blockIdx.x&7,t=threadIdx.x;
  if(t<64){ int b=t>>5,d=t&31; float s=0; for(int jj=0;jj<8;++jj) s+=((const float*)(p.ws+WS_CP))[(((br*16+h)*8+jj)*2+b)*256+j*32+d]; cx[b][d]=s; }
  __syncthreads();
  const int ch=t%56, rg=t/56;
  if (rg<4){ float a0[8]={0},a1[8]={0};
#pragma unroll
    for(int i=0;i<8;++i){ int d=rg+4*i; v4u w=*(const v4u*)(p.wo[br]+(size_t)(h*256+j*32+d)*M+ch*8); float x0=cx[0][d],x1=cx[1][d];
#pragma unroll
      for (int q=0;q<4;++q){ float lo=bflo(w[q]), hi=bfhi(w[q]); a0[2*q]+=x0*lo; a0[2*q+1]+=x0*hi; a1[2*q]+=x1*lo; a1[2*q+1]+=x1*hi; } }
#pragma unroll
    for(int q=0;q<8;++q){ red[rg][0][ch*8+q]=a0[q]; red[rg][1][ch*8+q]=a1[q]; } }
  __syncthreads();
  for(int i=t;i<896;i+=256){ int b=i/448,m=i%448; float s=red[0][b][m]+red[1][b][m]+red[2][b][m]+red[3][b][m]; ((float*)(p.ws+WS_OP))[((br*128+(blockIdx.x&127))*2+b)*448+m]=s; }
}
__global__ void __launch_bounds__(256) k6(P p) {
  __shared__ float red[4][64];
  const int br=blockIdx.x/14, ck=blockIdx.x%14, t=threadIdx.x, l=t&63, w=t>>6; const int i=ck*64+l;
  float s=0; const float* op=(const float*)(p.ws+WS_OP)+(size_t)br*128*896+i;
#pragma unroll 8
  for(int k=0;k<32;++k) s+=op[(size_t)(w*32+k)*896];
  red[w][l]=s; __syncthreads();
  if(t<64){ float v=red[0][l]+red[1][l]+red[2][l]+red[3][l]; int b=i/448,m=i%448; p.out[b*896+br*448+m]=f2bf(v); }
}
extern "C" void kernel_launch(void* const* d_in, const int* in_sizes, int n_in, void* d_out, int out_size, void* d_ws, size_t ws_size, hipStream_t stream) {
  P p; for(int br=0;br<2;++br){ p.x[br]=(const u16*)d_in[br*6+0]; p.ckv[br]=(const u16*)d_in[br*6+1]; p.wq[br]=(const u16*)d_in[br*6+2]; p.wuk[br]=(const u16*)d_in[br*6+3]; p.wuv[br]=(const u16*)d_in[br*6+4]; p.wo[br]=(const u16*)d_in[br*6+5]; }
  p.out=(u16*)d_out; p.ws=(unsigned char*)d_ws;
  const int lds3 = 64*1032*2 + 16*64*4;
  hipFuncSetAttribute((const void*)k3, hipFuncAttributeMaxDynamicSharedMemorySize, lds3);
  k1<<<256,256,0,stream>>>(p); k2<<<256,256,0,stream>>>(p); k3<<<256,256,lds3,stream>>>(p); k4<<<256,256,0,stream>>>(p); k5<<<256,256,0,stream>>>(p); k6<<<28,256,0,stream>>>(p);
}
```

```cpp
#include <hip/hip_runtime.h>
#include <stdint.h>

typedef unsigned v4u __attribute__((ext_vector_type(4)));
typedef unsigned v2u __attribute__((ext_vector_type(2)));
typedef float f32x4 __attribute__((ext_vector_type(4)));
typedef float f32x2 __attribute__((ext_vector_type(2)));
typedef short bf16x8 __attribute__((ext_vector_type(8)));
typedef short s16x4 __attribute__((ext_vector_type(4)));
typedef __bf16 bf16x2_t __attribute__((ext_vector_type(2)));
typedef unsigned long long u64;
#define LAS __attribute__((address_space(3)))
#define CAS __attribute__((address_space(4)))
#define DEV __device__ __forceinline__

struct Params { const void* in[12]; void* out; void* ws; };

constexpr unsigned WS_EPOCH = 0;
constexpr unsigned WS_C2 = 4096;
constexpr unsigned CS = 4224;
constexpr unsigned WS_CNT = WS_C2 + 256 * CS;
constexpr unsigned WS_QA = WS_CNT + 256 * CS;
constexpr unsigned WS_G3 = WS_QA + 131072;
constexpr unsigned WS_Q1 = WS_G3 + 262144;
constexpr unsigned WS_CP = WS_Q1 + 524288;
constexpr unsigned WS_OUTP = WS_CP + 2097152;
constexpr unsigned WS_OP = WS_OUTP + 1048576;
constexpr unsigned WS_BYTES = WS_OP + 8388608;
constexpr unsigned L_C = 0;
constexpr unsigned L_K = 81920;
constexpr unsigned L_S = 131072;
constexpr unsigned L_P1 = 147456;
constexpr unsigned L_QB = L_P1 + 8192;
constexpr unsigned L_CB = L_QB + 1024;
constexpr unsigned L_CBS = L_CB + 2048;
constexpr unsigned L_AF = L_CBS + 64;
constexpr unsigned L_FL = L_AF + 1024;
constexpr unsigned L_BYTES = L_FL + 128;
static_assert(L_BYTES <= 163840, "LDS");
enum { FL_PROG = 0, FL_P1 = 2, FL_Q = 3, FL_KFREE = 4, FL_GOV = 5, FL_CB = 6, FL_QA = 7, FL_S0 = 8, FL_TAG = 12, FL_OP = 14, FL_CLR = 15, FL_AF = 16, FL_H2 = 17, FL_W9 = 18, FL_W11 = 19 };

constexpr int AUX_NT = 2, AUX_SC1 = 16;
constexpr unsigned SPIN_G = 1u << 15, SPIN_L = 1u << 20;

#define WAITV(n) asm volatile("s_waitcnt vmcnt(%0)" ::"n"(n) : "memory")
#define WAITL() asm volatile("s_waitcnt lgkmcnt(0)" ::: "memory")
#define CFENCE() asm volatile("" ::: "memory")
#define SB() __builtin_amdgcn_sched_barrier(0)

DEV f32x4 mfma(v4u a, v4u b, f32x4 c) {
  return __builtin_amdgcn_mfma_f32_16x16x32_bf16(__builtin_bit_cast(bf16x8, a), __builtin_bit_cast(bf16x8, b), c, 0, 0, 0);
}
DEV unsigned pk(float a, float b) { f32x2 v = {a, b}; return __builtin_bit_cast(unsigned, __builtin_convertvector(v, bf16x2_t)); }
DEV float bflo(unsigned u) { return __uint_as_float(u << 16); }
DEV float bfhi(unsigned u) { return __uint_as_float(u & 0xffff0000u); }
DEV unsigned plo(unsigned a, unsigned b) { return __builtin_amdgcn_perm(b, a, 0x05040100u); }
DEV unsigned phi(unsigned a, unsigned b) { return __builtin_amdgcn_perm(b, a, 0x07060302u); }
DEV __amdgpu_buffer_rsrc_t rsrc(const void* p, unsigned bytes) { return __builtin_amdgcn_make_buffer_rsrc((void*)p, 0, (int)bytes, 0x00020000); }
DEV v4u ld_nt(__amdgpu_buffer_rsrc_t r, unsigned vo, unsigned so) { return __builtin_amdgcn_raw_buffer_load_b128(r, vo, so, AUX_NT); }
DEV v4u ld_sc(__amdgpu_buffer_rsrc_t r, unsigned vo, unsigned so) { return __builtin_amdgcn_raw_buffer_load_b128(r, vo, so, AUX_SC1); }
DEV void st_sc(v4u v, __amdgpu_buffer_rsrc_t r, unsigned vo, unsigned so) { __builtin_amdgcn_raw_buffer_store_b128(v, r, vo + so, 0, AUX_SC1); }
template <int MASK> DEV void cnt_add(__amdgpu_buffer_rsrc_t r, unsigned vo, unsigned one) {
  asm volatile("s_mov_b64 exec, %3\n\tbuffer_atomic_add %0, %1, %2, 0 offen sc1\n\ts_mov_b64 exec, -1" ::"v"(one), "v"(vo), "s"(r), "n"(MASK) : "memory");
}
template <int MASK> DEV unsigned cnt_read(__amdgpu_buffer_rsrc_t r, unsigned vo) {
  unsigned c = 0;
  asm volatile("s_mov_b64 exec, %3\n\tbuffer_atomic_add %0, %1, %2, 0 offen sc0 sc1\n\ts_mov_b64 exec, -1\n\ts_waitcnt vmcnt(0)" : "+v"(c) : "v"(vo), "s"(r), "n"(MASK) : "memory");
  return c;
}
template <int MASK> DEV void cnt_add64(__amdgpu_buffer_rsrc_t r, unsigned vo, u64 v) {
  asm volatile("s_mov_b64 exec, %3\n\tbuffer_atomic_add_x2 %0, %1, %2, 0 offen sc1\n\ts_mov_b64 exec, -1" ::"v"(v), "v"(vo), "s"(r), "n"(MASK) : "memory");
}
DEV u64 cnt_read64(__amdgpu_buffer_rsrc_t r, unsigned vo) {
  u64 c = 0;
  asm volatile("s_mov_b64 exec, 1\n\tbuffer_atomic_add_x2 %0, %1, %2, 0 offen sc0 sc1\n\ts_mov_b64 exec, -1\n\ts_waitcnt vmcnt(0)" : "+v"(c) : "v"(vo), "s"(r) : "memory");
  return ((u64)(unsigned)__builtin_amdgcn_readfirstlane((unsigned)(c >> 32)) << 32) | (unsigned)__builtin_amdgcn_readfirstlane((unsigned)c);
}
#define PIN8(f) do { _Pragma("unroll") for (int i_ = 0; i_ < 8; ++i_) asm volatile("" : "+v"(f[i_])); } while (0)
DEV unsigned badtag(v4u x, unsigned t) { return (x.y ^ t) | (x.w ^ t); }
constexpr int LWIN = 10, WWIN = 6;
DEV void dma4(__amdgpu_buffer_rsrc_t r, unsigned v0, unsigned v1, unsigned v2, unsigned v3, unsigned soff, unsigned lds) {
  asm volatile("s_mov_b32 m0, %6\n\ts_nop 0\n\t"
               "buffer_load_dwordx4 %0, %4, %5 offen nt lds\n\t"
               "s_waitcnt vmcnt(%7)\n\t"
               "buffer_load_dwordx4 %1, %4, %5 offen offset:1024 nt lds\n\t"
               "s_waitcnt vmcnt(%7)\n\t"
               "buffer_load_dwordx4 %2, %4, %5 offen offset:2048 nt lds\n\t"
               "s_waitcnt vmcnt(%7)\n\t"
               "buffer_load_dwordx4 %3, %4, %5 offen offset:3072 nt lds\n\t"
               "s_waitcnt vmcnt(%7)"
               :: "v"(v0), "v"(v1), "v"(v2), "v"(v3), "s"(r), "s"(soff), "s"(lds), "n"(LWIN - 1) : "memory");
}
constexpr int LAG = (LWIN + 2) / 4;
DEV void lds_wait_prog(LAS unsigned char* L, int v) {
  volatile LAS v2u* f = (volatile LAS v2u*)(L + L_FL + FL_PROG * 4);
#pragma unroll 1
  for (unsigned n = 0; n < SPIN_L; ++n) {
    const v2u x = *f;
    const int a = (int)x.x, b = (int)x.y;
    if (__builtin_amdgcn_readfirstlane(a < b ? a : b) >= v) break;
    __builtin_amdgcn_s_sleep(1);
  }
  CFENCE();
}
DEV void lds_wait_tight(LAS unsigned char* L, int fl, int v) {
  volatile LAS int* f = (volatile LAS int*)(L + L_FL) + fl;
#pragma unroll 1
  for (unsigned n = 0; n < 4 * SPIN_L; ++n) if (__builtin_amdgcn_readfirstlane(*f) >= v) break;
  CFENCE();
}
DEV void lds_wait_ge(LAS unsigned char* L, int fl, int v) {
  volatile LAS int* f = (volatile LAS int*)(L + L_FL) + fl;
#pragma unroll 1
  for (unsigned n = 0; n < SPIN_L; ++n) { if (__builtin_amdgcn_readfirstlane(*f) >= v) break; __builtin_amdgcn_s_sleep(1); }
  CFENCE();
}
DEV void lds_arrive(LAS unsigned char* L, int fl, int lane) {
  const unsigned ad = (unsigned)(size_t)(L + L_FL + fl * 4), one = lane == 0 ? 1u : 0u;
  asm volatile("s_mov_b64 exec, 1\n\tds_add_u32 %0, %1\n\ts_mov_b64 exec, -1" ::"v"(ad), "v"(one) : "memory");
}
DEV int lds_arrive_rtn(LAS unsigned char* L, int fl) {
  const unsigned ad = (unsigned)(size_t)(L + L_FL + fl * 4), one = 1u;
  unsigned old;
  asm volatile("s_mov_b64 exec, 1\n\tds_add_rtn_u32 %0, %1, %2\n\ts_mov_b64 exec, -1\n\ts_waitcnt lgkmcnt(0)" : "=&v"(old) : "v"(ad), "v"(one) : "memory");
  return __builtin_amdgcn_readfirstlane(old);
}
template <int CTRL> DEV float dpp(float x) { return __builtin_bit_cast(float, __builtin_amdgcn_mov_dpp(__builtin_bit_cast(int, x), CTRL, 0xf, 0xf, true)); }
constexpr int XOR1 = 0xB1, XOR2 = 0x4E, HMIR = 0x141, ROR8 = 0x128, ROR4 = 0x124;
DEV float sw16_sum(float a, float b) {
  auto s = __builtin_amdgcn_permlane16_swap(__float_as_uint(a), __float_as_uint(b), false, false);
  return __uint_as_float(s[0]) + __uint_as_float(s[1]);
}
DEV float sw32_sum(float a, float b) {
  auto s = __builtin_amdgcn_permlane32_swap(__float_as_uint(a), __float_as_uint(b), false, false);
  return __uint_as_float(s[0]) + __uint_as_float(s[1]);
}
DEV float sw16_max(float a) {
  auto s = __builtin_amdgcn_permlane16_swap(__float_as_uint(a), __float_as_uint(a), false, false);
  return fmaxf(__uint_as_float(s[0]), __uint_as_float(s[1]));
}
DEV float sw32_max(float a) {
  auto s = __builtin_amdgcn_permlane32_swap(__float_as_uint(a), __float_as_uint(a), false, false);
  return fmaxf(__uint_as_float(s[0]), __uint_as_float(s[1]));
}
DEV void tr8(const v4u (&w)[8], v4u (&f)[8]) {
#pragma unroll
  for (int e = 0; e < 4; ++e)
#pragma unroll
    for (int k = 0; k < 4; ++k) { f[2 * e][k] = plo(w[2 * k][e], w[2 * k + 1][e]); f[2 * e + 1][k] = phi(w[2 * k][e], w[2 * k + 1][e]); }
}
DEV v4u trfrag(LAS unsigned char* L, unsigned ad) {
  const v2u x0 = __builtin_bit_cast(v2u, __builtin_amdgcn_ds_read_tr16_b64_v4i16((LAS s16x4*)(L + ad)));
  const v2u x1 = __builtin_bit_cast(v2u, __builtin_amdgcn_ds_read_tr16_b64_v4i16((LAS s16x4*)(L + ad + 32768)));
  return v4u{x0.x, x0.y, x1.x, x1.y};
}

__global__ void __launch_bounds__(640, 3) mla_decode(Params p) {
  extern __shared__ __attribute__((aligned(16))) unsigned char smem[];
  LAS unsigned char* L = (LAS unsigned char*)smem;
  const int tid = threadIdx.x, lane = tid & 63, wv = __builtin_amdgcn_readfirstlane(tid >> 6);
  const int r = lane & 15, g = lane >> 4;
  const int bid = blockIdx.x, xcd = bid & 7;
  const int br = xcd >> 2, ii = ((xcd & 3) << 5) | (bid >> 3);
  const int h = ii >> 3, j = ii & 7;
  const int b = ii >> 6, kb = ii & 63;
  const __amdgpu_buffer_rsrc_t rws = rsrc(p.ws, WS_BYTES);

  if (wv >= 8) {
    const int lam = wv - 8;
    __builtin_amdgcn_s_setprio(3);
    if (lane < 32 && (lane & 1) == lam) ((LAS int*)(L + L_FL))[lane] = 0;
    const __amdgpu_buffer_rsrc_t rk = rsrc(p.in[br * 6 + 3], 8388608u), rc = rsrc(p.in[br * 6 + 1], 16777216u);
    volatile LAS int* prog = (volatile LAS int*)(L + L_FL) + FL_PROG + lam;
    {
      const int hi = lane >> 5;
      const unsigned vk = (hi * 8192 + (((lane & 31) ^ hi) << 4)) ^ (lam << 6);
      const unsigned v0 = vk, v1 = (vk ^ 32) + 15360, v2 = vk + 30720, v3 = (vk ^ 32) + 46080;
      unsigned so = (unsigned)(j * 128) * 8192u + h * 512 + lam * 65536, ld = L_K + lam * 4096;
      WAITL();
      __builtin_amdgcn_s_barrier();
#pragma unroll 1
      for (int n = 0; n < 4; ++n) {
        dma4(rk, v0, v1, v2, v3, so, ld);
        *prog = 2 * n + 1 - LAG;
        dma4(rk, v0 ^ 128, v1 ^ 128, v2 ^ 128, v3 ^ 128, so + 131072, ld + 8192);
        *prog = 2 * n + 2 - LAG;
        so += 262144; ld += 16384;
      }
    }
    {
      unsigned vc[8];
#pragma unroll
      for (int k = 0; k < 8; ++k) {
        const int key = 4 * (k >> 1) + 2 * lam + (k & 1);
        vc[k] = (unsigned)(lane ^ ((key << 1) & 15) ^ ((key & 8) ? 9 : 0)) << 4;
      }
      unsigned so = (unsigned)(b * 4096 + kb * 64) * 2048u + lam * 4096, ld = L_C + lam * 4096;
#pragma unroll 1
      for (int t = 0; t < 4; ++t) {
#pragma unroll
        for (int k = 0; k < 4; ++k) {
          if (k == 2 && t == 2) lds_wait_ge(L, FL_KFREE, 4);
          dma4(rc, vc[2 * k], vc[2 * k], vc[2 * k + 1], vc[2 * k + 1], so + k * 8192, ld + k * 8192);
          *prog = 9 + 4 * t + k - LAG;
        }
        so += 32768; ld += 32768;
      }
    }
    lds_arrive(L, FL_GOV, lane);
    WAITV(0);
    *prog = 24;
    return;
  }

  if (wv >= 4) {
    const int ww = wv - 4;
    __builtin_amdgcn_s_setprio(1);
    const __amdgpu_buffer_rsrc_t rq = rsrc(p.in[br * 6 + 2], 3670016u);
    unsigned x0[7], x1[7];
    {
      const CAS unsigned* xp = (const CAS unsigned*)p.in[br * 6 + 0] + 28 * j + 7 * ww;
#pragma unroll
      for (int n = 0; n < 7; ++n) { x0[n] = xp[n]; x1[n] = xp[224 + n]; }
    }
    v4u a[8];
    {
      const unsigned vo = (unsigned)(lane >> 5) * 8192u + (lane & 31) * 16;
      const unsigned so = (unsigned)(56 * j + 14 * ww) * 8192u + h * 512;
#pragma unroll
      for (int n = 0; n < 7; ++n) a[n] = ld_nt(rq, vo, so + n * 16384);
      a[7] = v4u{0, 0, 0, 0};
    }
    const v2u ep = __builtin_amdgcn_raw_buffer_load_b64(rws, 0, WS_EPOCH + br * 256, AUX_SC1);
    SB();
    __builtin_amdgcn_s_barrier();
    SB();
    v4u xf;
    {
      unsigned xs[8];
      const bool b1 = (r >> 3) != 0;
#pragma unroll
      for (int n = 0; n < 7; ++n) xs[n] = b1 ? x1[n] : x0[n];
      xs[7] = 0;
      const unsigned sel = (g >> 1) ? 0x07060302u : 0x05040100u;
      const bool on = (g & 1) == ((r >> 2) & 1);
#pragma unroll
      for (int k = 0; k < 4; ++k) { const unsigned v = __builtin_amdgcn_perm(xs[2 * k + 1], xs[2 * k], sel); xf[k] = on ? v : 0u; }
    }
    {
      v4u f[8];
      tr8(a, f);
      f32x4 q0, q1;
#pragma unroll
      for (int jj = 0; jj < 8; ++jj) {
        const f32x4 d = mfma(xf, f[jj], f32x4{0, 0, 0, 0});
        if (jj < 4) q0[jj] = d[0]; else q1[jj - 4] = d[0];
      }
      LAS f32x4* dst = (LAS f32x4*)(L + L_P1 + ww * 2048 + lane * 32);
      dst[0] = q0; dst[1] = q1;
    }
    const u64 tag = (((u64)ep.y << 32) | ep.x) + 1;
    const unsigned T = (unsigned)tag, V = (T << 6) | 1u;
    if (ww == 0 && lane == 0) *(LAS v2u*)(L + L_FL + FL_TAG * 4) = v2u{T, (unsigned)(tag >> 32)};
    lds_arrive(L, FL_P1, lane);
    const int pc = 32 * ww + (lane & 31);
    lds_wait_ge(L, FL_CLR, 1);
    lds_wait_ge(L, FL_P1, 4);
    {
      const LAS f32x4* sp = (const LAS f32x4*)(L + L_P1 + pc * 16 + (lane >> 5) * 4096);
      const f32x4 s = sp[0] + sp[128];
      const float s0 = sw32_sum(s[0], s[0]), s1 = sw32_sum(s[1], s[1]), s2 = sw32_sum(s[2], s[2]), s3 = sw32_sum(s[3], s[3]);
      if (lane < 32) st_sc(v4u{pk(s0, s1), T, pk(s2, s3), T}, rws, pc * 16, WS_Q1 + ((br * 16 + h) * 8 + j) * 2048);
    }
    const __amdgpu_buffer_rsrc_t rv = rsrc(p.in[br * 6 + 4], 8388608u), ro = rsrc(p.in[br * 6 + 5], 3670016u);
    v4u fv0[8], fv1[8], fo[8];
    {
      v4u w0[8], w1[8], wo[8];
      const unsigned vo = (unsigned)(lane >> 5) * 8192u + (lane & 31) * 16;
      const unsigned so = (unsigned)(128 * j + 32 * ww) * 8192u + h * 512;
      const unsigned voo = (16 * ww + r) < 56 ? (unsigned)(8 * g) * 896u + (16 * ww + r) * 16 : 0x80000000u;
      const unsigned soo = (unsigned)(h * 256 + j * 32) * 896u;
      lds_wait_prog(L, 4);
      v4u q[4];
#pragma unroll
      for (int k = 0; k < 4; ++k) q[k] = ld_sc(rws, pc * 16 + (lane >> 5) * 2048, WS_Q1 + ((br * 16 + h) * 8 + 2 * k) * 2048);
      SB();
      if (!__all((badtag(q[0], T) | badtag(q[1], T) | badtag(q[2], T) | badtag(q[3], T)) == 0)) {
        _Pragma("unroll 1") for (unsigned n = 0; n < SPIN_G; ++n) {
          CFENCE();
#pragma unroll
          for (int k = 0; k < 4; ++k) q[k] = ld_sc(rws, pc * 16 + (lane >> 5) * 2048, WS_Q1 + ((br * 16 + h) * 8 + 2 * k) * 2048);
          if (__all((badtag(q[0], T) | badtag(q[1], T) | badtag(q[2], T) | badtag(q[3], T)) == 0)) break;
          __builtin_amdgcn_s_sleep(4);
        }
      }
      {
        float s0 = (bflo(q[0].x) + bflo(q[1].x)) + (bflo(q[2].x) + bflo(q[3].x)), s1 = (bfhi(q[0].x) + bfhi(q[1].x)) + (bfhi(q[2].x) + bfhi(q[3].x));
        float s2 = (bflo(q[0].z) + bflo(q[1].z)) + (bflo(q[2].z) + bflo(q[3].z)), s3 = (bfhi(q[0].z) + bfhi(q[1].z)) + (bfhi(q[2].z) + bfhi(q[3].z));
        s0 = sw32_sum(s0, s2); s1 = sw32_sum(s1, s3);
        *(LAS unsigned*)(L + L_QB + pc * 8 + (lane >> 5) * 4) = pk(s0, s1);
      }
      lds_arrive(L, FL_Q, lane);
      SB();
      {
        v4u wf[16];
        lds_wait_prog(L, 2 * ww + 2);
        const unsigned base = L_K + (32 * ww + 8 * (r >> 2) + (r & 3)) * 512 + ((g ^ r) << 4);
#pragma unroll
        for (int s = 0; s < 8; ++s)
#pragma unroll
          for (int tt = 0; tt < 2; ++tt) wf[2 * s + tt] = *(LAS v4u*)(L + ((base ^ ((s & 3) << 6)) + (s >> 2) * 256 + tt * 2048));
        lds_arrive(L, FL_KFREE, lane);
        lds_wait_ge(L, FL_Q, 4);
        v4u qf[8];
#pragma unroll
        for (int s = 0; s < 8; ++s) qf[s] = *(LAS v4u*)(L + L_QB + (r & 1) * 512 + s * 64 + g * 16);
        SB();
        f32x4 acc[2] = {f32x4{0, 0, 0, 0}, f32x4{0, 0, 0, 0}};
#pragma unroll
        for (int s = 0; s < 8; ++s)
#pragma unroll
          for (int tt = 0; tt < 2; ++tt) acc[tt] = mfma(wf[2 * s + tt], qf[s], acc[tt]);
        constexpr float SC = 0.0625f * 1.4426950408889634f;
        const v4u o = {pk(acc[0][0] * SC, acc[0][1] * SC), pk(acc[0][2] * SC, acc[0][3] * SC), pk(acc[1][0] * SC, acc[1][1] * SC), pk(acc[1][2] * SC, acc[1][3] * SC)};
        if (r < 2) st_sc(o, rws, r * 32768 + g * 16, WS_QA + br * 65536 + (4 * j + ww) * 1024 + h * 64);
      }
      SB();
      WAITV(0);
      if (lds_arrive_rtn(L, FL_QA) == 3) cnt_add<0xff>(rws, WS_C2 + ((br * 16 + h) * 8 + (lane & 7)) * CS, V);
      lds_wait_ge(L, FL_GOV, 2);
#define HOLD() do { if (__builtin_amdgcn_readfirstlane(*((volatile LAS int*)(L + L_FL) + FL_S0 + 3)) >= 1) lds_wait_ge(L, FL_OP, 4); } while (0)
#pragma unroll
      for (int n = 0; n < 8; ++n) { HOLD(); w0[n] = ld_nt(rv, vo, so + n * 16384); SB(); if (n >= WWIN - 1) WAITV(WWIN - 1); }
#pragma unroll
      for (int n = 0; n < 8; ++n) { HOLD(); w1[n] = ld_nt(rv, vo, so + n * 16384 + 131072); SB(); WAITV(WWIN - 1); }
      tr8(w0, fv0);
      PIN8(fv0);
#pragma unroll
      for (int n = 0; n < 8; ++n) { HOLD(); wo[n] = ld_nt(ro, voo, soo + n * 896); SB(); if (n < 7) WAITV(WWIN - 1); }
      tr8(w1, fv1);
      PIN8(fv1);
      SB();
      lds_wait_tight(L, FL_CB, 4);
      const int bb = r >> 3;
      const LAS f32x4* c0 = (const LAS f32x4*)(L + L_CB + bb * 1024 + (4 * ww + (g >> 1)) * 32);
      const f32x4 y0 = c0[0], y1 = c0[1], y2 = c0[32], y3 = c0[33], y4 = c0[4], y5 = c0[5], y6 = c0[36], y7 = c0[37];
      const f32x4 sv = *(LAS f32x4*)(L + L_CBS + bb * 16);
      const unsigned on = (g & 1) == ((r >> 2) & 1) ? 0xffffffffu : 0u;
      const float M = fmaxf(sv[0], sv[2]);
      float f0 = __builtin_amdgcn_exp2f(sv[0] - M), f1 = __builtin_amdgcn_exp2f(sv[2] - M);
      const float inv = __builtin_amdgcn_rcpf(f0 * sv[1] + f1 * sv[3]);
      f0 *= inv; f1 *= inv;
      const f32x4 z0 = y0 * f0 + y2 * f1, z1 = y1 * f0 + y3 * f1, z2 = y4 * f0 + y6 * f1, z3 = y5 * f0 + y7 * f1;
      const v4u cf0 = v4u{pk(z0[0], z0[1]), pk(z0[2], z0[3]), pk(z1[0], z1[1]), pk(z1[2], z1[3])} & on;
      const v4u cf1 = v4u{pk(z2[0], z2[1]), pk(z2[2], z2[3]), pk(z3[0], z3[1]), pk(z3[2], z3[3])} & on;
      f32x4 d[8];
#pragma unroll
      for (int jj = 0; jj < 8; ++jj) d[jj] = mfma(cf0, fv0[jj], f32x4{0, 0, 0, 0});
#pragma unroll
      for (int jj = 0; jj < 8; ++jj) d[jj] = mfma(cf1, fv1[jj], d[jj]);
      const unsigned cvo = ((lane & 31) >> 2) * 8192 + (lane >> 5) * 64 + (lane & 3) * 16;
      const unsigned cso = WS_CP + (br * 128 + h * 8) * 8192 + (4 * j + ww) * 256;
      st_sc(v4u{pk(d[0][0], d[1][0]), T, pk(d[2][0], d[3][0]), T}, rws, cvo, cso);
      st_sc(v4u{pk(d[4][0], d[5][0]), T, pk(d[6][0], d[7][0]), T}, rws, cvo, cso + 128);
      cnt_add<1>(rws, WS_CNT + (br * 128 + 64 + h) * CS, 1u);
      lds_arrive(L, FL_W9, lane);
      tr8(wo, fo);
      PIN8(fo);
    }
    {
      lds_wait_tight(L, FL_AF, 1);
      const v4u af = *(LAS v4u*)(L + L_AF + lane * 16);
      f32x4 d[8];
#pragma unroll
      for (int jj = 0; jj < 8; ++jj) d[jj] = mfma(af, fo[jj], f32x4{0, 0, 0, 0});
      if (g < 2 && 16 * ww + r < 56) {
        const unsigned vo2 = (14 * g + 4 * ww + (r >> 2)) * 16384 + (r & 3) * 16;
        st_sc(v4u{pk(d[0][0], d[1][0]), T, pk(d[2][0], d[3][0]), T}, rws, vo2, WS_OUTP + br * 458752 + ii * 64);
        st_sc(v4u{pk(d[4][0], d[5][0]), T, pk(d[6][0], d[7][0]), T}, rws, vo2, WS_OUTP + br * 458752 + ii * 64 + 8192);
      }
      cnt_add<1>(rws, WS_CNT + (br * 128 + 80 + ww * 4 + (ii >> 5)) * CS, 1u);
      lds_arrive(L, FL_W11, lane);
    }
    return;
  }

  {
    const int w = wv;
    __builtin_amdgcn_s_setprio(2);
    if (w == 0 && lane < 8) st_sc(v4u{0, 0, 0, 0}, rws, (lane & 3) * 16 + (lane < 4 ? WS_C2 : WS_CNT), (br * 128 + ii) * CS);
    __builtin_amdgcn_s_barrier();
    if (w == 0) {
      WAITV(0);
      lds_arrive(L, FL_CLR, lane);
    }
    lds_wait_ge(L, FL_QA, 4);
    const v2u tg = *(volatile LAS v2u*)(L + L_FL + FL_TAG * 4);
    const unsigned T = tg.x, V = (T << 6) | 1u;
    if (w == 0) {
      _Pragma("unroll 1") for (unsigned n = 0; n < SPIN_G; ++n) {
        const unsigned c = cnt_read<0xffff>(rws, WS_C2 + ((br * 16 + r) * 8 + j) * CS);
        if ((__ballot(c == 8 * V) & 0xffff) == 0xffff) break;
        __builtin_amdgcn_s_sleep(4);
      }
      lds_arrive(L, FL_H2, lane);
    } else lds_wait_ge(L, FL_H2, 1);
    v4u qa[8];
#pragma unroll
    for (int s = 0; s < 8; ++s) qa[s] = ld_sc(rws, r * 64 + g * 16, WS_QA + (br * 2 + b) * 32768 + (8 * w + s) * 1024);
    SB();
    const int fr = ((r << 1) & 15) ^ ((r & 8) ? 9 : 0);
    const unsigned rbase = L_C + r * 2048 + ((g ^ fr) << 4) + w * 512;
    const int key = 4 * g + (r >> 2), pp = r & 3;
    const int fk = ((key << 1) & 15) ^ ((key & 8) ? 9 : 0);
    const unsigned tbase = L_C + key * 2048 + (((pp >> 1) ^ fk) << 4) + 8 * (pp & 1) + w * 512;
    v4u k0[16];
    f32x4 S[4];
    unsigned ta[8];
#pragma unroll
    for (int t = 0; t < 4; ++t) {
      lds_wait_prog(L, 8 + 4 * (t + 1));
      v4u cf[8];
#pragma unroll
      for (int s = 0; s < 8; ++s) cf[s] = *(LAS v4u*)(L + ((rbase ^ ((s & 3) << 6)) + (s >> 2) * 256 + t * 32768));
      SB();
      f32x4 a0 = {0, 0, 0, 0}, a1 = {0, 0, 0, 0};
#pragma unroll
      for (int s = 0; s < 8; s += 2) { a0 = mfma(cf[s], qa[s], a0); a1 = mfma(cf[s + 1], qa[s + 1], a1); }
      *(LAS f32x4*)(L + L_S + (w * 4 + t) * 1024 + lane * 16) = a0 + a1;
      lds_arrive(L, FL_S0 + t, lane);
      if (t == 2) {
#pragma unroll
        for (int ct = 0; ct < 16; ++ct) k0[ct] = trfrag(L, (tbase ^ ((ct & 7) << 5)) + (ct >> 3) * 256);
#pragma unroll
        for (int c8 = 0; c8 < 8; ++c8) { ta[c8] = (tbase ^ (c8 << 5)) + 65536; asm volatile("" : "+v"(ta[c8])); }
        SB();
      }
      if (t >= 2) {
#pragma unroll
        for (int t2 = (t == 2 ? 0 : 3); t2 <= t; ++t2) {
          lds_wait_tight(L, FL_S0 + t2, 4);
          const LAS f32x4* sp = (const LAS f32x4*)(L + L_S + t2 * 1024 + lane * 16);
          S[t2] = (sp[0] + sp[256]) + (sp[512] + sp[768]);
          if (t == 2) asm volatile("" : "+v"(S[t2]));
        }
      }
    }
    SB();
    v4u k1[16];
#pragma unroll
    for (int ct = 0; ct < 16; ++ct) k1[ct] = trfrag(L, ta[ct & 7] + (ct >> 3) * 256);
    SB();
    float m = fmaxf(fmaxf(S[0][0], S[0][1]), fmaxf(S[0][2], S[0][3]));
#pragma unroll
    for (int t = 1; t < 4; ++t) m = fmaxf(m, fmaxf(fmaxf(S[t][0], S[t][1]), fmaxf(S[t][2], S[t][3])));
    m = sw32_max(sw16_max(m));
#pragma unroll
    for (int t = 0; t < 4; ++t)
#pragma unroll
      for (int e = 0; e < 4; ++e) S[t][e] = __builtin_amdgcn_exp2f(S[t][e] - m);
    v4u pf[2];
#pragma unroll
    for (int u = 0; u < 2; ++u) pf[u] = v4u{pk(S[2 * u][0], S[2 * u][1]), pk(S[2 * u][2], S[2 * u][3]), pk(S[2 * u + 1][0], S[2 * u + 1][1]), pk(S[2 * u + 1][2], S[2 * u + 1][3])};
    SB();
    {
      const unsigned ovo = (4 * g) * 262144 + r * 16;
      const unsigned oso = WS_OP + br * 4194304 + (2 * w) * 32768 + (b * 64 + kb) * 256;
#pragma unroll
      for (int hf = 0; hf < 2; ++hf) {
        f32x4 o[8];
#pragma unroll
        for (int k = 0; k < 8; ++k) o[k] = mfma(pf[0], k0[8 * hf + k], f32x4{0, 0, 0, 0});
#pragma unroll
        for (int k = 0; k < 8; ++k) o[k] = mfma(pf[1], k1[8 * hf + k], o[k]);
#pragma unroll
        for (int e = 0; e < 4; ++e)
          st_sc(v4u{pk(o[0][e], o[1][e]), pk(o[2][e], o[3][e]), pk(o[4][e], o[5][e]), pk(o[6][e], o[7][e])}, rws, ovo, oso + hf * 32768 + e * 262144);
      }
    }
    {
      const f32x4 l4 = (S[0] + S[1]) + (S[2] + S[3]);
      float l = (l4[0] + l4[1]) + (l4[2] + l4[3]);
      l = sw16_sum(l, l); l = sw32_sum(l, l);
      if (w == 0) st_sc(v4u{__float_as_uint(m), __float_as_uint(l), __float_as_uint(m), __float_as_uint(l)}, rws, r * 8192 + g * 16, WS_G3 + br * 131072 + (b * 64 + kb) * 64);
      if (ii == 0 && w == 1 && lane < 4) st_sc(v4u{tg.x, tg.y, tg.x, tg.y}, rws, lane * 16, WS_EPOCH + br * 256);
      WAITV(0);
      if (lds_arrive_rtn(L, FL_OP) == 3) cnt_add64<0xffff>(rws, WS_CNT + (br * 128 + r * 2 + b) * CS, 1ull << (kb & 56));
    }
    const int bA = w >> 1, kq = 32 * (w & 1);
    v4u pl[8];
    v2u sg[8];
    {
      unsigned done = 0;
      const unsigned pso = WS_OP + (br * 128 + ii) * 32768 + (bA * 64 + kq) * 256, sso = WS_G3 + (br * 16 + h) * 8192 + (bA * 64 + kq) * 64;
      _Pragma("unroll 1") for (unsigned n = 0; n < SPIN_G; ++n) {
        const u64 c64 = cnt_read64(rws, WS_CNT + (br * 128 + h * 2 + bA) * CS);
        const unsigned c = n + 1 < SPIN_G ? (unsigned)(c64 >> (kq)) : 0x08080808u;
#pragma unroll
        for (int q = 0; q < 4; ++q)
          if (((c >> (8 * q)) & 255u) == 8u && !(done & (1u << q))) {
            done |= 1u << q;
#pragma unroll
            for (int i = 2 * q; i < 2 * q + 2; ++i) {
              pl[i] = ld_sc(rws, lane * 16, pso + i * 1024);
              sg[i] = __builtin_amdgcn_raw_buffer_load_b64(rws, g * 64, sso + i * 256, AUX_SC1);
            }
          }
        if (done == 15u) break;
      }
    }
    {
      float Mw = __uint_as_float(sg[0].x);
#pragma unroll
      for (int i = 1; i < 8; ++i) Mw = fmaxf(Mw, __uint_as_float(sg[i].x));
      Mw = sw32_max(sw16_max(Mw));
      float ev[8], Lw = 0;
#pragma unroll
      for (int i = 0; i < 8; ++i) { ev[i] = __builtin_amdgcn_exp2f(__uint_as_float(sg[i].x) - Mw); Lw += ev[i] * __uint_as_float(sg[i].y); }
      Lw = sw16_sum(Lw, Lw); Lw = sw32_sum(Lw, Lw);
      *(LAS f32x2*)(L + L_CBS + w * 8) = f32x2{Mw, Lw};
      float acc[8] = {0, 0, 0, 0, 0, 0, 0, 0};
#pragma unroll
      for (int i = 0; i < 8; ++i) {
        const float ei = ev[i];
#pragma unroll
        for (int e2 = 0; e2 < 4; ++e2) { acc[2 * e2] += ei * bflo(pl[i][e2]); acc[2 * e2 + 1] += ei * bfhi(pl[i][e2]); }
      }
      const float r0 = sw32_sum(acc[0], acc[4]), r1 = sw32_sum(acc[1], acc[5]), r2 = sw32_sum(acc[2], acc[6]), r3 = sw32_sum(acc[3], acc[7]);
      LAS float* cb = (LAS float*)(L + L_CB + w * 512 + g * 128 + (r & 1) * 32 + (r >> 1) * 4);
      cb[0] = sw16_sum(r0, r2); cb[16] = sw16_sum(r1, r3);
    }
    lds_arrive(L, FL_CB, lane);
    if (w == 1) {
      lds_wait_ge(L, FL_W9, 4);
      _Pragma("unroll 1") for (unsigned n = 0; n < SPIN_G; ++n) {
        if (__builtin_amdgcn_readfirstlane(cnt_read<1>(rws, WS_CNT + (br * 128 + 64 + h) * CS)) >= 28) break;
      }
      v4u c[8];
      const unsigned vo = ((r & 3) + 4 * (r >> 3)) * 256 + ((r >> 2) & 1) * 64 + g * 16;
      _Pragma("unroll 1") for (unsigned n = 0; n < SPIN_G; ++n) {
        CFENCE();
        unsigned bad = 0;
#pragma unroll
        for (int k = 0; k < 8; ++k) c[k] = ld_sc(rws, vo, WS_CP + (br * 128 + ii) * 8192 + (k >> 1) * 2048 + (k & 1) * 128);
#pragma unroll
        for (int k = 0; k < 8; ++k) bad |= badtag(c[k], T);
        if (__all(bad == 0)) break;
        __builtin_amdgcn_s_sleep(1);
      }
      float s[8];
#pragma unroll
      for (int hf = 0; hf < 2; ++hf) {
        s[4 * hf + 0] = (bflo(c[hf].x) + bflo(c[2 + hf].x)) + (bflo(c[4 + hf].x) + bflo(c[6 + hf].x));
        s[4 * hf + 1] = (bfhi(c[hf].x) + bfhi(c[2 + hf].x)) + (bfhi(c[4 + hf].x) + bfhi(c[6 + hf].x));
        s[4 * hf + 2] = (bflo(c[hf].z) + bflo(c[2 + hf].z)) + (bflo(c[4 + hf].z) + bflo(c[6 + hf].z));
        s[4 * hf + 3] = (bfhi(c[hf].z) + bfhi(c[2 + hf].z)) + (bfhi(c[4 + hf].z) + bfhi(c[6 + hf].z));
      }
#pragma unroll
      for (int e = 0; e < 8; ++e) { s[e] += dpp<XOR1>(s[e]); s[e] += dpp<XOR2>(s[e]); s[e] += dpp<ROR8>(s[e]); }
      *(LAS v4u*)(L + L_AF + lane * 16) = v4u{pk(s[0], s[1]), pk(s[2], s[3]), pk(s[4], s[5]), pk(s[6], s[7])};
      lds_arrive(L, FL_AF, lane);
    }
    if ((ii & 1) == 0 && (ii >> 1) < 56 && w == 0) {
      const int red = ii >> 1;
      {
        const int rd = red >> 1, wq = (rd >= 14 ? rd - 14 : rd) >> 2;
        lds_wait_ge(L, FL_W11, 4);
        _Pragma("unroll 1") for (unsigned n = 0; n < SPIN_G; ++n) {
          const unsigned c = cnt_read<15>(rws, WS_CNT + (br * 128 + 80 + wq * 4 + (lane & 3)) * CS);
          if ((__ballot(c >= 28) & 15) == 15) break;
        }
      }
      v4u x[8];
      _Pragma("unroll 1") for (unsigned n = 0; n < SPIN_G; ++n) {
        CFENCE();
        unsigned bad = 0;
#pragma unroll
        for (int k = 0; k < 8; ++k) x[k] = ld_sc(rws, lane * 16, WS_OUTP + br * 458752 + red * 8192 + k * 1024);
#pragma unroll
        for (int k = 0; k < 8; ++k) bad |= badtag(x[k], T);
        if (__all(bad == 0)) break;
        __builtin_amdgcn_s_sleep(1);
      }
      float s[4] = {0, 0, 0, 0};
#pragma unroll
      for (int k = 0; k < 8; ++k) { s[0] += bflo(x[k].x); s[1] += bfhi(x[k].x); s[2] += bflo(x[k].z); s[3] += bfhi(x[k].z); }
#pragma unroll
      for (int e = 0; e < 4; ++e) { s[e] += dpp<ROR4>(s[e]); s[e] += dpp<ROR8>(s[e]); s[e] = sw16_sum(s[e], s[e]); s[e] = sw32_sum(s[e], s[e]); }
      if (lane < 4) {
        const int rd = red >> 1, bo = rd >= 14, mo = (rd - 14 * bo) * 32 + lane * 8 + (red & 1) * 4;
        *(v2u*)((unsigned short*)p.out + bo * 896 + br * 448 + mo) = v2u{pk(s[0], s[1]), pk(s[2], s[3])};
      }
    }
  }
}

extern "C" void kernel_launch(void* const* d_in, const int* in_sizes, int n_in, void* d_out, int out_size, void* d_ws, size_t ws_size, hipStream_t stream) {
  Params p;
  for (int i = 0; i < 12; ++i) p.in[i] = d_in[i];
  p.out = d_out; p.ws = d_ws;
  (void)hipFuncSetAttribute((const void*)mla_decode, hipFuncAttributeMaxDynamicSharedMemorySize, L_BYTES);
  mla_decode<<<256, 640, L_BYTES, stream>>>(p);
}
```
